```python
import torch

def _softmax(x, axis=-1):
    return torch.softmax(x, dim=axis)
def _gelu(x):
    return torch.nn.functional.gelu(x, approximate="tanh")
def _silu(x):
    return torch.nn.functional.silu(x)
def _sigmoid(x):
    return torch.sigmoid(x)
def _layernorm(x, axis=-1, eps=1e-5):
    m = x.mean(dim=axis, keepdim=True)
    v = x.var(dim=axis, keepdim=True, unbiased=False)
    return (x - m) / torch.sqrt(v + eps)
def _scatter_add(tgt, idx, src, axis=0):
    return tgt.clone().index_add_(axis, idx, src)
def _segment_sum(src, seg, n):
    out = torch.zeros((n,) + src.shape[1:], dtype=src.dtype)
    return out.index_add_(0, seg, src)
def _pad(x, pw):
    flat = tuple(v for pair in reversed(pw) for v in pair)
    return torch.nn.functional.pad(x, flat)

kernel_name = 'gated_mix__gqa_block_cached__gqa_block_cached__decode'

def setup_inputs():

    _w = (lambda a: (lambda t: t.to(torch.bfloat16) if t.is_floating_point() else t)(torch.as_tensor(a)))
    def _A_setup():
        B, Sq, D = 1, 1, 640
        Hq, Hkv, Sk, Dh = 32, 32, 16384, 128
        assert Hq % Hkv == 0, (Hq, Hkv)
        return {
            "x": _w((lambda *s: torch.randn(*s, dtype=torch.bfloat16) * 0.05)(B, Sq, D)),
            "k_cache": _w((lambda *s: torch.randn(*s, dtype=torch.bfloat16) * 3.2)(B, Hkv, Sk, Dh)),
            "v_cache": _w((lambda *s: torch.randn(*s, dtype=torch.bfloat16) * 0.05)(B, Hkv, Sk, Dh)),
            "wq": _w((lambda *s: torch.randn(*s, dtype=torch.bfloat16) * 3.2)(D, Hq * Dh) * (D ** -0.5)),
            "wo": _w((lambda *s: torch.randn(*s, dtype=torch.bfloat16) * 0.05)(Hq * Dh, D) * ((Hq * Dh) ** -0.5)),
        }
    _A_in = _A_setup()

    def _B_setup():
        B, Sq, D = 1, 1, 640
        Hq, Hkv, Sk, Dh = 32, 32, 16384, 128
        assert Hq % Hkv == 0, (Hq, Hkv)
        return {
            "x": _w((lambda *s: torch.randn(*s, dtype=torch.bfloat16) * 0.05)(B, Sq, D)),
            "k_cache": _w((lambda *s: torch.randn(*s, dtype=torch.bfloat16) * 3.2)(B, Hkv, Sk, Dh)),
            "v_cache": _w((lambda *s: torch.randn(*s, dtype=torch.bfloat16) * 0.05)(B, Hkv, Sk, Dh)),
            "wq": _w((lambda *s: torch.randn(*s, dtype=torch.bfloat16) * 3.2)(D, Hq * Dh) * (D ** -0.5)),
            "wo": _w((lambda *s: torch.randn(*s, dtype=torch.bfloat16) * 0.05)(Hq * Dh, D) * ((Hq * Dh) ** -0.5)),
        }
    _B_in = _B_setup()

    return {
        "A_x": _A_in["x"],
        "A_k_cache": _A_in["k_cache"],
        "A_v_cache": _A_in["v_cache"],
        "A_wq": _A_in["wq"],
        "A_wo": _A_in["wo"],
        "B_x": _B_in["x"],
        "B_k_cache": _B_in["k_cache"],
        "B_v_cache": _B_in["v_cache"],
        "B_wq": _B_in["wq"],
        "B_wo": _B_in["wo"],
        "gate_w": _w((lambda *s: torch.randn(*s, dtype=torch.bfloat16) * 0.05)(640) * 16.0),
    }

def reference(A_x, A_k_cache, A_v_cache, A_wq, A_wo, B_x, B_k_cache, B_v_cache, B_wq, B_wo, gate_w):
    def _A_ref(x, k_cache, v_cache, wq, wo):
        B, Sq, D = x.shape
        Hq, Dh = 32, 128
        Hkv = k_cache.shape[1]
        rep = Hq // Hkv
        q = (lambda x, *d: x.permute(*d))((x @ wq).reshape(B, Sq, Hq, Dh), 0, 2, 1, 3)
        k = (lambda x, n, axis: torch.repeat_interleave(x, n, dim=axis))(k_cache, rep, axis=1)
        v = (lambda x, n, axis: torch.repeat_interleave(x, n, dim=axis))(v_cache, rep, axis=1)
        scores = torch.einsum("bhqd,bhkd->bhqk", q, k) * (Dh ** -0.5)
        p = _softmax(scores, axis=-1)
        o = torch.einsum("bhqk,bhkd->bhqd", p, v)
        return (lambda x, *d: x.permute(*d))(o, 0, 2, 1, 3).reshape(B, Sq, Hq * Dh) @ wo

    def _B_ref(x, k_cache, v_cache, wq, wo):
        B, Sq, D = x.shape
        Hq, Dh = 32, 128
        Hkv = k_cache.shape[1]
        rep = Hq // Hkv
        q = (lambda x, *d: x.permute(*d))((x @ wq).reshape(B, Sq, Hq, Dh), 0, 2, 1, 3)
        k = (lambda x, n, axis: torch.repeat_interleave(x, n, dim=axis))(k_cache, rep, axis=1)
        v = (lambda x, n, axis: torch.repeat_interleave(x, n, dim=axis))(v_cache, rep, axis=1)
        scores = torch.einsum("bhqd,bhkd->bhqk", q, k) * (Dh ** -0.5)
        p = _softmax(scores, axis=-1)
        o = torch.einsum("bhqk,bhkd->bhqd", p, v)
        return (lambda x, *d: x.permute(*d))(o, 0, 2, 1, 3).reshape(B, Sq, Hq * Dh) @ wo

    x = A_x
    g = _sigmoid((x * gate_w).sum(-1, keepdims=True))
    a = _A_ref(x, A_k_cache, A_v_cache, A_wq, A_wo)
    b = _B_ref(x, B_k_cache, B_v_cache, B_wq, B_wo)
    return g * a + (1 - g) * b
```

```cpp
#include <hip/hip_runtime.h>
#include <hip/hip_bf16.h>
#include <stdint.h>

#define DYN 1
#define TK 8
#define SLEEP1 32
#define SLEEP2 20
#define STAMPS 0

#define GAS __attribute__((address_space(1)))
#define LAS __attribute__((address_space(3)))
typedef unsigned v4u __attribute__((ext_vector_type(4)));
typedef float v4f __attribute__((ext_vector_type(4)));
typedef float v2f __attribute__((ext_vector_type(2)));
typedef __bf16 bf16x2 __attribute__((ext_vector_type(2)));
typedef __amdgpu_buffer_rsrc_t rsrc_t;

namespace {
constexpr int DM = 640, NH = 32, SK = 16384, DH = 128;
constexpr int NWG = 256, NTHR = 512, NWAVE = 8;
constexpr int KEYS = 4 * TK;
constexpr int ITEM_BYTES = KEYS * DH * 2;
constexpr int NITEMS = SK / KEYS;
constexpr int QWAVES = 64;
constexpr int GITEMS = 2 * NITEMS;
constexpr int HEAD_BYTES = SK * DH * 2;
constexpr int RSRC3 = 0x00020000;
constexpr int AUX_NT = 2, AUX_SC1 = 16;
constexpr unsigned SPIN_CAP = 1u << 20;

constexpr int WS_EPOCH = 0;
constexpr int WS_CTR = 4096;
constexpr int WS_Q = 32768;
constexpr int Q_MEMBER = 2 * 64 * 16;
constexpr int WS_P = WS_Q + 32 * 8 * Q_MEMBER;
constexpr int P_REC = 72 * 16;
constexpr int WS_Y = WS_P + 32 * 2 * 8 * P_REC;
constexpr int Y_REC = 320 * 16;
constexpr int WS_STAMP = WS_Y + 64 * Y_REC;
constexpr int NSTAMP = 96;
constexpr int WS_END = WS_STAMP + (STAMPS ? NWG * NWAVE * NSTAMP * 8 : 0);

constexpr int L_WO = 0;
constexpr int L_A = 128 * 160 * 2;
constexpr int REC = 132;
constexpr int L_B = L_A + 16 * REC * 4;
constexpr int L_C = L_B + 8 * 256 * 4;
constexpr int L_RED = L_A;
constexpr int L_G = L_C + 1024;
constexpr int L_END = L_G + 256;

struct Params {
  const void* x; const void* kA; const void* vA; const void* wqA; const void* woA;
  const void* kB; const void* vB; const void* wqB; const void* woB; const void* gw;
  void* out; void* ws;
};

__device__ __forceinline__ float bf2f_lo(unsigned u) { return __builtin_bit_cast(float, u << 16); }
__device__ __forceinline__ float bf2f_hi(unsigned u) { return __builtin_bit_cast(float, u & 0xffff0000u); }
__device__ __forceinline__ float dot2(unsigned k, unsigned q, float acc) {
  return __builtin_amdgcn_fdot2_f32_bf16(__builtin_bit_cast(bf16x2, k), __builtin_bit_cast(bf16x2, q), acc, false);
}
__device__ __forceinline__ float rbf(float f) { return (float)(__bf16)f; }
__device__ __forceinline__ unsigned pack_bf(float lo, float hi) {
  bf16x2 v = {(__bf16)lo, (__bf16)hi};
  return __builtin_bit_cast(unsigned, v);
}
template <int CTRL> __device__ __forceinline__ float dpp(float x) {
  return __builtin_bit_cast(float, __builtin_amdgcn_mov_dpp(__builtin_bit_cast(int, x), CTRL, 0xf, 0xf, true));
}
template <int CTRL, int ROWMASK> __device__ __forceinline__ float dpp_keep(float x) {
  const int xi = __builtin_bit_cast(int, x);
  return __builtin_bit_cast(float, __builtin_amdgcn_update_dpp(xi, xi, CTRL, ROWMASK, 0xf, false));
}
constexpr int XOR1 = 0xB1, XOR2 = 0x4E, XOR7 = 0x141, XOR8 = 0x128, BC15 = 0x142, BC31 = 0x143, NEWBC = 0x150;
__device__ __forceinline__ float xrow_sum(float x) {
  auto s = __builtin_amdgcn_permlane16_swap(__float_as_uint(x), __float_as_uint(x), false, false);
  x = __uint_as_float(s[0]) + __uint_as_float(s[1]);
  auto t = __builtin_amdgcn_permlane32_swap(__float_as_uint(x), __float_as_uint(x), false, false);
  return __uint_as_float(t[0]) + __uint_as_float(t[1]);
}
__device__ __forceinline__ float row_sum16(float x) {
  x += dpp<XOR1>(x); x += dpp<XOR2>(x); x += dpp<XOR7>(x); x += dpp<XOR8>(x);
  return x;
}
__device__ __forceinline__ v4u ld_nt(rsrc_t r, int voff, int soff) { return __builtin_amdgcn_raw_buffer_load_b128(r, voff, soff, AUX_NT); }
__device__ __forceinline__ v4u ld_sc1(rsrc_t r, int voff) { return __builtin_amdgcn_raw_buffer_load_b128(r, voff, 0, AUX_SC1); }
__device__ __forceinline__ void st_unit(rsrc_t r, int voff, float a, float b, unsigned t0, unsigned t1) {
  v4u v = {__float_as_uint(a), t0, __float_as_uint(b), t1};
  __builtin_amdgcn_raw_buffer_store_b128(v, r, voff, 0, AUX_SC1);
}
__device__ __forceinline__ bool unit_ok(const v4u& v, unsigned t0, unsigned t1) { return v.y == t0 && v.w == t1; }
#define SCHED_FENCE() __builtin_amdgcn_sched_barrier(0)
template <int N> __device__ __forceinline__ void pin(float (&v)[N]) {
  if constexpr (N == 4) asm volatile("" : "+v"(v[0]), "+v"(v[1]), "+v"(v[2]), "+v"(v[3]) :: "memory");
  else if constexpr (N == 8) asm volatile("" : "+v"(v[0]), "+v"(v[1]), "+v"(v[2]), "+v"(v[3]), "+v"(v[4]), "+v"(v[5]), "+v"(v[6]), "+v"(v[7]) :: "memory");
  else asm volatile("" : "+v"(v[0]), "+v"(v[1]), "+v"(v[2]), "+v"(v[3]), "+v"(v[4]), "+v"(v[5]), "+v"(v[6]), "+v"(v[7]),
                    "+v"(v[8]), "+v"(v[9]), "+v"(v[10]), "+v"(v[11]), "+v"(v[12]), "+v"(v[13]), "+v"(v[14]), "+v"(v[15]) :: "memory");
  __builtin_amdgcn_sched_barrier(0);
}
constexpr int DUP = 16 / TK;
__device__ __forceinline__ void load_slot(v4u (&R)[TK], rsrc_t rs, int vo, int so) {
#pragma unroll
  for (int i = 0; i < TK; ++i) R[i] = ld_nt(rs, vo + (i >> 2) * 4096 + (i & 3) * 1024, so);
}
template <int N, int CTRL, int BIT> __device__ __forceinline__ void halve(float (&x)[TK], int c) {
#pragma unroll
  for (int h = 0; h < N / 2; ++h) { const bool hi = c & BIT; const float a = hi ? x[h + N / 2] : x[h], o = hi ? x[h] : x[h + N / 2]; x[h] = a + dpp<CTRL>(o); }
}
__device__ __forceinline__ float reduce_scores(float (&x)[TK], int c) {
  if constexpr (TK == 16) { halve<16, XOR8, 8>(x, c); halve<8, XOR7, 4>(x, c); halve<4, XOR2, 2>(x, c); halve<2, XOR1, 1>(x, c); return x[0]; }
  else if constexpr (TK == 8) { halve<8, XOR8, 8>(x, c); halve<4, XOR7, 4>(x, c); halve<2, XOR2, 2>(x, c); return x[0] + dpp<XOR1>(x[0]); }
  else { halve<4, XOR8, 8>(x, c); halve<2, XOR7, 4>(x, c); float t = x[0] + dpp<XOR2>(x[0]); return t + dpp<XOR1>(t); }
}
template <int I> __device__ __forceinline__ void pv_all(const v4u (&V)[TK], float pr, float (&acc)[8]) {
  if constexpr (I < TK) {
    const float pb = dpp<NEWBC + DUP * I>(pr);
#pragma unroll
    for (int k = 0; k < 4; ++k) { acc[2 * k] = fmaf(pb, bf2f_lo(V[I][k]), acc[2 * k]); acc[2 * k + 1] = fmaf(pb, bf2f_hi(V[I][k]), acc[2 * k + 1]); }
    pv_all<I + 1>(V, pr, acc);
  }
}
#define STAMP(i) do { } while (0)

__global__ void __launch_bounds__(NTHR, 2) fused_kernel(Params p) {
  extern __shared__ __attribute__((aligned(16))) unsigned char smem[];
  LAS unsigned char* L = (LAS unsigned char*)smem;
  const int tid = threadIdx.x, lane = tid & 63, w = __builtin_amdgcn_readfirstlane(tid >> 6);
  const int b = blockIdx.x, g = b >> 3, j = b & 7;
  const int br = w >> 2, wq4 = w & 3;
  const int r = lane >> 4, c = lane & 15;
  const rsrc_t rsW = __builtin_amdgcn_make_buffer_rsrc(p.ws, 0, WS_END, RSRC3);
  STAMP(0);
  const unsigned epoch = *(const GAS unsigned*)((const char*)p.ws + WS_EPOCH);
  const unsigned tag0 = epoch + 1u, tag1 = (epoch + 1u) * 0x9E3779B1u ^ 0x5bd1e995u;

  if (j == 0 && w == 0) {
    __builtin_amdgcn_raw_buffer_store_b32(0u, rsW, lane == 0 ? WS_CTR + g * 256 : 0x7ffffff0, 0, AUX_SC1);
  }

  const char* wq = (const char*)(br ? p.wqB : p.wqA);
  const rsrc_t rsQ = __builtin_amdgcn_make_buffer_rsrc((void*)wq, 0, DM * NH * DH * 2, RSRC3);
  const rsrc_t rsX = __builtin_amdgcn_make_buffer_rsrc((void*)p.x, 0, DM * 2, RSRC3);
  const rsrc_t rsG = __builtin_amdgcn_make_buffer_rsrc((void*)p.gw, 0, DM * 2, RSRC3);
  const int row0 = 80 * j + 20 * wq4 + r;
  unsigned xv[5]; v4u wv[5];
#pragma unroll
  for (int i = 0; i < 5; ++i) xv[i] = __builtin_amdgcn_raw_buffer_load_b16(rsX, (row0 + 4 * i) * 2, 0, 0);
  const int goff = lane < 40 ? (80 * w + 2 * lane) * 2 : 0x7ffffff0;
  const unsigned gx = __builtin_amdgcn_raw_buffer_load_b32(rsX, goff, 0, 0);
  const unsigned gg = __builtin_amdgcn_raw_buffer_load_b32(rsG, goff, 0, 0);
#pragma unroll
  for (int i = 0; i < 5; ++i) wv[i] = ld_nt(rsQ, (row0 + 4 * i) * (NH * DH * 2) + g * (DH * 2) + c * 16, 0);
  SCHED_FENCE();
  if (SLEEP1 > 0) __builtin_amdgcn_s_sleep(SLEEP1);
  SCHED_FENCE();

  const char* kA = (const char*)p.kA + (size_t)g * HEAD_BYTES; const char* kB = (const char*)p.kB + (size_t)g * HEAD_BYTES;
  const char* vA = (const char*)p.vA + (size_t)g * HEAD_BYTES; const char* vB = (const char*)p.vB + (size_t)g * HEAD_BYTES;
  const int vo = lane * 16;
  const int qw = j * 8 + w;
  const rsrc_t rsKA = __builtin_amdgcn_make_buffer_rsrc((void*)kA, 0, HEAD_BYTES, RSRC3);
  const rsrc_t rsVA = __builtin_amdgcn_make_buffer_rsrc((void*)vA, 0, HEAD_BYTES, RSRC3);
  v4u KX[TK], VX[TK], K0[TK], V0[TK];
  load_slot(KX, rsKA, vo, qw * ITEM_BYTES);
  SCHED_FENCE();

  {
    float a[8];
#pragma unroll
    for (int k = 0; k < 8; ++k) a[k] = 0.f;
#pragma unroll
    for (int i = 0; i < 5; ++i) {
      const float xf = bf2f_lo(xv[i]);
#pragma unroll
      for (int k = 0; k < 4; ++k) { a[2 * k] = fmaf(xf, bf2f_lo(wv[i][k]), a[2 * k]); a[2 * k + 1] = fmaf(xf, bf2f_hi(wv[i][k]), a[2 * k + 1]); }
    }
#pragma unroll
    for (int k = 0; k < 8; ++k) a[k] = xrow_sum(a[k]);
    LAS float* qw_l = (LAS float*)(L + L_A) + w * REC;
    if (r == 0) {
      *(LAS v4f*)(qw_l + 8 * c) = v4f{a[0], a[1], a[2], a[3]};
      *(LAS v4f*)(qw_l + 8 * c + 4) = v4f{a[4], a[5], a[6], a[7]};
    }
    float gs = rbf(bf2f_lo(gx) * bf2f_lo(gg)) + rbf(bf2f_hi(gx) * bf2f_hi(gg));
    gs = xrow_sum(row_sum16(gs));
    if (lane == 0) ((LAS float*)(L + L_G))[w] = gs;
  }
  STAMP(1);
  __syncthreads();
  if (w < 2) {
    const LAS float* s = (const LAS float*)(L + L_A) + (4 * w) * REC + 2 * lane;
    const float v0 = (s[0] + s[REC]) + (s[2 * REC] + s[3 * REC]);
    const float v1 = (s[1] + s[REC + 1]) + (s[2 * REC + 1] + s[3 * REC + 1]);
    st_unit(rsW, WS_Q + (g * 8 + j) * Q_MEMBER + (w * 64 + lane) * 16, v0, v1, tag0, tag1);
  }
  STAMP(2);
  {
    const char* qsrc = (const char*)p.ws + WS_Q + (g * 8 + w) * Q_MEMBER;
    v4u u0, u1;
    if (SLEEP2 > 0) __builtin_amdgcn_s_sleep(SLEEP2);
    asm volatile("global_load_dwordx4 %0, %2, %3 sc1\n\tglobal_load_dwordx4 %1, %2, %3 offset:1024 sc1" : "=&v"(u0), "=&v"(u1) : "v"(vo), "s"(qsrc) : "memory");
    SCHED_FENCE();
    __builtin_amdgcn_s_barrier();
    SCHED_FENCE();
    load_slot(VX, rsVA, vo, qw * ITEM_BYTES);
    load_slot(K0, rsKA, vo, (qw + QWAVES) * ITEM_BYTES);
    SCHED_FENCE();
    asm volatile("s_waitcnt vmcnt(%c2)" : "+v"(u0), "+v"(u1) : "i"(2 * TK) : "memory");
    SCHED_FENCE();
    STAMP(11);
    const bool ok1 = __all(unit_ok(u0, tag0, tag1) && unit_ok(u1, tag0, tag1));
    if (!ok1) asm volatile("global_load_dwordx4 %0, %2, %3 sc1\n\tglobal_load_dwordx4 %1, %2, %3 offset:1024 sc1" : "+v"(u0), "+v"(u1) : "v"(vo), "s"(qsrc) : "memory");
    SCHED_FENCE();
    load_slot(V0, rsVA, vo, (qw + QWAVES) * ITEM_BYTES);
    SCHED_FENCE();
    unsigned spins = 0;
    if (!ok1) {
      asm volatile("s_waitcnt vmcnt(%c2)" : "+v"(u0), "+v"(u1) : "i"(TK) : "memory");
      spins = 1;
      while (!__all(unit_ok(u0, tag0, tag1) && unit_ok(u1, tag0, tag1))) {
        if (++spins > SPIN_CAP) break;
        __builtin_amdgcn_s_sleep(1);
        asm volatile("global_load_dwordx4 %0, %2, %3 sc1\n\tglobal_load_dwordx4 %1, %2, %3 offset:1024 sc1\n\ts_waitcnt vmcnt(0)" : "=&v"(u0), "=&v"(u1) : "v"(vo), "s"(qsrc) : "memory");
      }
    }
    LAS float* qp = (LAS float*)(L + L_B) + w * 256;
    *(LAS v2f*)(qp + 2 * lane) = v2f{__uint_as_float(u0.x), __uint_as_float(u0.z)};
    *(LAS v2f*)(qp + 128 + 2 * lane) = v2f{__uint_as_float(u1.x), __uint_as_float(u1.z)};
  }
  STAMP(3);
  __syncthreads();
  if (tid < 128) {
    const LAS float* s = (const LAS float*)(L + L_B) + 2 * tid;
    float v0 = 0.f, v1 = 0.f;
#pragma unroll
    for (int m = 0; m < 8; ++m) { v0 += s[m * 256]; v1 += s[m * 256 + 1]; }
    ((LAS unsigned*)(L + L_C))[tid] = pack_bf(v0, v1);
  }
  __syncthreads();
  v4u q = *(const LAS v4u*)(L + L_C + c * 16);
  {
    LAS float* rec = (LAS float*)(L + L_A) + (8 + w) * REC;
    if (lane < 33) *(LAS v4f*)(rec + 4 * lane) = v4f{lane == 0 ? -INFINITY : 0.f, 0.f, 0.f, 0.f};
  }

  {
    const char* wo = (const char*)((j >> 2) ? p.woB : p.woA) + (size_t)g * DH * DM * 2 + (j & 3) * 320;
#pragma unroll
    for (int i = 0; i < 5; ++i) {
      const int piece = (w * 5 + i) * 64 + lane;
      const char* src = wo + (piece / 20) * (DM * 2) + (piece % 20) * 16;
      const int dst = L_WO + (w * 5 + i) * 1024;
      unsigned keep;
      asm volatile("s_mov_b32 %0, m0\n\ts_mov_b32 m0, %2\n\ts_nop 0\n\tglobal_load_lds_dwordx4 %1, off nt\n\ts_mov_b32 m0, %0"
                   : "=&s"(keep) : "v"(src), "s"(dst) : "memory");
    }
  }
  STAMP(4);

  constexpr float C2 = 0.08838834764831845f * 1.4426950408889634f;
  const rsrc_t rsC = __builtin_amdgcn_make_buffer_rsrc((char*)p.ws + WS_CTR + g * 256, 0, 4, RSRC3);
  const int coff = lane == 0 ? 0 : 0x7ffffff0;
  float m = -INFINITY, l = 0.f, acc[8];
#pragma unroll
  for (int k = 0; k < 8; ++k) acc[k] = 0.f;
  auto flush = [&](int slot) {
    float a[8];
#pragma unroll
    for (int k = 0; k < 8; ++k) a[k] = xrow_sum(acc[k]);
    const float ls = (1.f / DUP) * xrow_sum(row_sum16(l));
    LAS float* rec = (LAS float*)(L + L_A) + slot * REC;
    *(LAS v4f*)(rec + 4 + 8 * c + 4 * (r & 1)) = (r & 1) ? v4f{a[4], a[5], a[6], a[7]} : v4f{a[0], a[1], a[2], a[3]};
    *(LAS v2f*)rec = v2f{m, ls};
  };
  constexpr int NSTATIC = 4;
  bool inB = false;
  auto qk = [&](const v4u (&K)[TK], float (&x)[TK]) __attribute__((always_inline)) {
#pragma unroll
    for (int i = 0; i < TK; ++i) {
      float d = dot2(K[i][0], q[0], 0.f);
      d = dot2(K[i][1], q[1], d); d = dot2(K[i][2], q[2], d); x[i] = dot2(K[i][3], q[3], d);
    }
  };
  auto finish = [&](float (&x)[TK], const v4u (&V)[TK]) __attribute__((always_inline)) {
    const float s = reduce_scores(x, c) * C2;
    float t = s;
    if (DUP <= 2) t = fmaxf(t, dpp<XOR2>(t));
    if (DUP <= 1) t = fmaxf(t, dpp<XOR1>(t));
    t = fmaxf(t, dpp<XOR7>(t)); t = fmaxf(t, dpp<XOR8>(t));
    t = fmaxf(t, dpp_keep<BC15, 0xa>(t)); t = fmaxf(t, dpp_keep<BC31, 0xc>(t));
    const float mt = __builtin_bit_cast(float, __builtin_amdgcn_readlane(__builtin_bit_cast(int, t), 63));
    const float mn = fmaxf(m, mt);
    const float alpha = __builtin_amdgcn_exp2f(m - mn);
    const float pr = __builtin_amdgcn_exp2f(s - mn);
    m = mn;
    l = fmaf(l, alpha, pr);
#pragma unroll
    for (int k = 0; k < 8; ++k) acc[k] *= alpha;
    pv_all<0>(V, pr, acc);
  };
  auto item_off = [&](int it) { return it < GITEMS ? (it & (NITEMS - 1)) * ITEM_BYTES : HEAD_BYTES; };
  int cur = qw + QWAVES, nx = qw + 2 * QWAVES;
  int tk = qw - QWAVES;
  {
    float x0[TK], xx[TK];
    qk(K0, x0);
    pin(x0);
    load_slot(K0, rsKA, vo, item_off(nx));
    SCHED_FENCE();
    qk(KX, xx);
    finish(xx, VX);
    finish(x0, V0);
    pin(acc);
    load_slot(V0, rsVA, vo, item_off(nx));
    SCHED_FENCE();
    cur = nx; nx = qw + 3 * QWAVES;
    tk = __builtin_amdgcn_raw_ptr_buffer_atomic_add_i32(1, rsC, coff, 0, 0);
  }
  int nstat = NSTATIC;
  for (;;) {
    const bool nxB = nx >= NITEMS;
    const int so = item_off(nx);
    if (cur >= NITEMS && !inB) {
      flush(w);
      inB = true; m = -INFINITY; l = 0.f;
#pragma unroll
      for (int k = 0; k < 8; ++k) acc[k] = 0.f;
      q = *(const LAS v4u*)(L + L_C + 256 + c * 16);
    }
    float x[TK];
    qk(K0, x);
    pin(x);
    load_slot(K0, __builtin_amdgcn_make_buffer_rsrc((void*)(nxB ? kB : kA), 0, HEAD_BYTES, RSRC3), vo, so);
    SCHED_FENCE();
    finish(x, V0);
    pin(acc);
    load_slot(V0, __builtin_amdgcn_make_buffer_rsrc((void*)(nxB ? vB : vA), 0, HEAD_BYTES, RSRC3), vo, so);
    SCHED_FENCE();
    cur = nx;
    if (cur >= GITEMS) break;
    nx = __builtin_amdgcn_readfirstlane(tk) + NSTATIC * QWAVES;
    tk = __builtin_amdgcn_raw_ptr_buffer_atomic_add_i32(1, rsC, coff, 0, 0);
    SCHED_FENCE();
  }
  STAMP(5);

  flush(inB ? 8 + w : w);
  asm volatile("s_waitcnt vmcnt(0)" ::: "memory");
  __syncthreads();
  STAMP(6);
  if (w < 2) {
    const LAS float* rec = (const LAS float*)(L + L_A) + (8 * w) * REC;
    float M = rec[0];
#pragma unroll
    for (int k = 1; k < 8; ++k) M = fmaxf(M, rec[k * REC]);
    const float Ms = M == -INFINITY ? 0.f : M;
    float Ls = 0.f, o0 = 0.f, o1 = 0.f;
#pragma unroll
    for (int k = 0; k < 8; ++k) {
      const float wt = __builtin_amdgcn_exp2f(rec[k * REC] - Ms);
      Ls = fmaf(wt, rec[k * REC + 1], Ls); o0 = fmaf(wt, rec[k * REC + 4 + 2 * lane], o0); o1 = fmaf(wt, rec[k * REC + 5 + 2 * lane], o1);
    }
    const int po = WS_P + ((g * 2 + w) * 8 + j) * P_REC;
    st_unit(rsW, po + lane * 16, o0, o1, tag0, tag1);
    st_unit(rsW, lane == 0 ? po + 64 * 16 : 0x7ffffff0, M, Ls, tag0, tag1);
  }
  {
    const int po = WS_P + ((g * 2 + (j >> 2)) * 8 + w) * P_REC;
    unsigned spins = 0;
    v4u u0, u1;
    for (;;) {
      u0 = ld_sc1(rsW, po + lane * 16); u1 = ld_sc1(rsW, po + 64 * 16);
      if (__all(unit_ok(u0, tag0, tag1) && unit_ok(u1, tag0, tag1))) break;
      if (++spins > SPIN_CAP) break;
      __builtin_amdgcn_s_sleep(1);
    }
    LAS float* rec = (LAS float*)(L + L_B) + w * REC;
    *(LAS v2f*)(rec + 4 + 2 * lane) = v2f{__uint_as_float(u0.x), __uint_as_float(u0.z)};
    if (lane == 0) { rec[0] = __uint_as_float(u1.x); rec[1] = __uint_as_float(u1.z); }
  }
  STAMP(7);
  __syncthreads();
  if (tid < 128) {
    const LAS float* rec = (const LAS float*)(L + L_B);
    float M = rec[0];
#pragma unroll
    for (int k = 1; k < 8; ++k) M = fmaxf(M, rec[k * REC]);
    float Ls = 0.f, o = 0.f;
#pragma unroll
    for (int k = 0; k < 8; ++k) { const float wt = __builtin_amdgcn_exp2f(rec[k * REC] - M); Ls = fmaf(wt, rec[k * REC + 1], Ls); o = fmaf(wt, rec[k * REC + 4 + tid], o); }
    ((LAS float*)(L + L_C))[tid] = o / Ls;
  }
  __syncthreads();
  if (tid < 320) {
    const int ch = tid % 20, rg = tid / 20;
    const LAS float* of = (const LAS float*)(L + L_C) + rg * 8;
    const v4f oa = *(const LAS v4f*)of, ob = *(const LAS v4f*)(of + 4);
    float a[8];
#pragma unroll
    for (int k = 0; k < 8; ++k) a[k] = 0.f;
#pragma unroll
    for (int i = 0; i < 8; ++i) {
      const v4u wv2 = *(const LAS v4u*)(L + L_WO + (rg * 8 + i) * 320 + ch * 16);
      const float ov = i < 4 ? oa[i] : ob[i - 4];
#pragma unroll
      for (int k = 0; k < 4; ++k) { a[2 * k] = fmaf(ov, bf2f_lo(wv2[k]), a[2 * k]); a[2 * k + 1] = fmaf(ov, bf2f_hi(wv2[k]), a[2 * k + 1]); }
    }
    LAS float* red = (LAS float*)(L + L_RED) + rg * 160 + ch * 8;
    *(LAS v4f*)red = v4f{a[0], a[1], a[2], a[3]};
    *(LAS v4f*)(red + 4) = v4f{a[4], a[5], a[6], a[7]};
  }
  __syncthreads();
  if (tid < 80) {
    const LAS float* red = (const LAS float*)(L + L_RED) + 2 * tid;
    float y0 = 0.f, y1 = 0.f;
#pragma unroll
    for (int k = 0; k < 16; ++k) { y0 += red[k * 160]; y1 += red[k * 160 + 1]; }
    st_unit(rsW, WS_Y + ((j >> 2) * 32 + g) * Y_REC + ((j & 3) * 80 + tid) * 16, y0, y1, tag0, tag1);
  }
  STAMP(8);
  if (b < 40) {
    const int yo = WS_Y + (tid >> 3) * Y_REC + (b * 8 + (tid & 7)) * 16;
    unsigned spins = 0;
    v4u u;
    for (;;) {
      u = ld_sc1(rsW, yo);
      if (__all(unit_ok(u, tag0, tag1))) break;
      if (++spins > SPIN_CAP) break;
      __builtin_amdgcn_s_sleep(1);
    }
    float y0 = __uint_as_float(u.x), y1 = __uint_as_float(u.z);
    y0 += dpp<XOR8>(y0); y1 += dpp<XOR8>(y1);
    y0 = xrow_sum(y0); y1 = xrow_sum(y1);
    LAS float* red = (LAS float*)(L + L_C) + w * 16;
    if (lane < 8) *(LAS v2f*)(red + 2 * lane) = v2f{y0, y1};
    __syncthreads();
    if (tid < 8) {
      const LAS float* rr = (const LAS float*)(L + L_C) + 2 * tid;
      const LAS float* gp = (const LAS float*)(L + L_G);
      float gs = 0.f;
#pragma unroll
      for (int k = 0; k < 8; ++k) gs += gp[k];
      const float gate = rbf(1.f / (1.f + __expf(-rbf(gs)))), ngate = rbf(1.f - gate);
      float a0 = (rr[0] + rr[16]) + (rr[32] + rr[48]), a1 = (rr[1] + rr[17]) + (rr[33] + rr[49]);
      float b0 = (rr[64] + rr[80]) + (rr[96] + rr[112]), b1 = (rr[65] + rr[81]) + (rr[97] + rr[113]);
      const float r0 = rbf(gate * rbf(a0)) + rbf(ngate * rbf(b0)), r1 = rbf(gate * rbf(a1)) + rbf(ngate * rbf(b1));
      ((GAS unsigned*)p.out)[b * 8 + tid] = pack_bf(r0, r1);
      if (b == 0 && tid == 0) __hip_atomic_store((GAS unsigned*)((char*)p.ws + WS_EPOCH), tag0, __ATOMIC_RELAXED, __HIP_MEMORY_SCOPE_AGENT);
    }
  }
  STAMP(9);
}
}

extern "C" void kernel_launch(void* const* d_in, const int* in_sizes, int n_in,
                              void* d_out, int out_size, void* d_ws, size_t ws_size,
                              hipStream_t stream) {
  Params p;
  p.x = d_in[0]; p.kA = d_in[1]; p.vA = d_in[2]; p.wqA = d_in[3]; p.woA = d_in[4];
  p.kB = d_in[6]; p.vB = d_in[7]; p.wqB = d_in[8]; p.woB = d_in[9]; p.gw = d_in[10];
  p.out = d_out; p.ws = d_ws;
  hipLaunchKernelGGL(fused_kernel, dim3(NWG), dim3(NTHR), L_END, stream, p);
}
```

```cpp
#include <hip/hip_runtime.h>
#include <hip/hip_bf16.h>
#include <stdint.h>

#define GAS __attribute__((address_space(1)))
#define LAS __attribute__((address_space(3)))
typedef unsigned v4u __attribute__((ext_vector_type(4)));
typedef float v4f __attribute__((ext_vector_type(4)));
typedef float v2f __attribute__((ext_vector_type(2)));
typedef __bf16 bf16x2 __attribute__((ext_vector_type(2)));
typedef __amdgpu_buffer_rsrc_t rsrc_t;
typedef unsigned v2u __attribute__((ext_vector_type(2)));
#define CAS __attribute__((address_space(4)))

namespace {
constexpr int DM = 640, NH = 32, SK = 16384, DH = 128;
constexpr int NWG = 256, NTHR = 512, NWAVE = 8;
constexpr int TK = 16;
constexpr int KEYS = 4 * TK;
constexpr int ITEM_BYTES = KEYS * DH * 2;
constexpr int NSPLIT = 4;
constexpr int WITEMS = SK / NSPLIT / KEYS / NWAVE;
constexpr int HEAD_BYTES = SK * DH * 2;
constexpr int RSRC3 = 0x00020000;
constexpr int AUX_NT = 2, AUX_SC1 = 16;
constexpr int OOB = 0x7ffffff0;
constexpr unsigned SPIN_CAP = 1u << 20;
constexpr double PACE_TBS = 7.35;
constexpr unsigned DELTA0 = 90;
constexpr unsigned POLL_AT = 400;
constexpr unsigned SIGMA256 = (unsigned)((double)ITEM_BYTES * NWG * NWAVE / (PACE_TBS * 1e12) * 1e8 * 256.0 + 0.5);

constexpr int WS_EPOCH = 0;
constexpr int WS_Q = 4096;
constexpr int Q_REC = 64 * 16;
constexpr int WS_P = WS_Q + 64 * 4 * Q_REC;
constexpr int P_REC = 72 * 16;
constexpr int WS_Y = WS_P + 64 * 4 * P_REC;
constexpr int Y_REC = 320 * 16;
constexpr int WS_END = WS_Y + 64 * Y_REC;

constexpr int REC = 132;
constexpr int L_WO = 0;
constexpr int L_A = 128 * 160 * 2;
constexpr int L_C = L_A + 8 * REC * 4;
constexpr int L_RED = L_C + 1024;
constexpr int L_G = L_RED + 8 * 160 * 4;
constexpr int L_SH = L_G + 32;
constexpr int L_END = L_SH + 32;

struct Params {
  const void* wqA; const void* wqB; const void* kA; const void* kB; const void* vA; const void* vB; const void* x; void* ws;
  const void* woA; const void* woB; const void* gw; void* out;
};

__device__ __forceinline__ float bf2f_lo(unsigned u) { return __builtin_bit_cast(float, u << 16); }
__device__ __forceinline__ float bf2f_hi(unsigned u) { return __builtin_bit_cast(float, u & 0xffff0000u); }
__device__ __forceinline__ float dot2(unsigned k, unsigned q, float acc) {
  return __builtin_amdgcn_fdot2_f32_bf16(__builtin_bit_cast(bf16x2, k), __builtin_bit_cast(bf16x2, q), acc, false);
}
__device__ __forceinline__ float rbf(float f) {
  float r = (float)(__bf16)f;
  asm("" : "+v"(r));
  return r;
}
__device__ __forceinline__ unsigned pack_bf(float lo, float hi) {
  bf16x2 v = {(__bf16)lo, (__bf16)hi};
  return __builtin_bit_cast(unsigned, v);
}
template <int CTRL> __device__ __forceinline__ float dpp(float x) {
  return __builtin_bit_cast(float, __builtin_amdgcn_mov_dpp(__builtin_bit_cast(int, x), CTRL, 0xf, 0xf, true));
}
template <int CTRL, int ROWMASK> __device__ __forceinline__ float dpp_keep(float x) {
  const int xi = __builtin_bit_cast(int, x);
  return __builtin_bit_cast(float, __builtin_amdgcn_update_dpp(xi, xi, CTRL, ROWMASK, 0xf, false));
}
constexpr int XOR1 = 0xB1, XOR2 = 0x4E, XOR7 = 0x141, XOR8 = 0x128, BC15 = 0x142, BC31 = 0x143, NEWBC = 0x150;
__device__ __forceinline__ float xrow_sum(float x) {
  auto s = __builtin_amdgcn_permlane16_swap(__float_as_uint(x), __float_as_uint(x), false, false);
  x = __uint_as_float(s[0]) + __uint_as_float(s[1]);
  auto t = __builtin_amdgcn_permlane32_swap(__float_as_uint(x), __float_as_uint(x), false, false);
  return __uint_as_float(t[0]) + __uint_as_float(t[1]);
}
__device__ __forceinline__ float row_sum16(float x) {
  x += dpp<XOR1>(x); x += dpp<XOR2>(x); x += dpp<XOR7>(x); x += dpp<XOR8>(x);
  return x;
}
__device__ __forceinline__ v4u ld_nt(rsrc_t r, int voff, int soff) { return __builtin_amdgcn_raw_buffer_load_b128(r, voff, soff, AUX_NT); }
__device__ __forceinline__ v4u ld_sc1(rsrc_t r, int voff) { return __builtin_amdgcn_raw_buffer_load_b128(r, voff, 0, AUX_SC1); }
__device__ __forceinline__ void st_unit(rsrc_t r, int voff, float a, float b, unsigned t0, unsigned t1) {
  v4u v = {__float_as_uint(a), t0, __float_as_uint(b), t1};
  __builtin_amdgcn_raw_buffer_store_b128(v, r, voff, 0, AUX_SC1);
}
__device__ __forceinline__ bool unit_ok(const v4u& v, unsigned t0, unsigned t1) { return v.y == t0 && v.w == t1; }
__device__ __forceinline__ void poll_unit(rsrc_t rs, int off, unsigned t0, unsigned t1, float& x, float& z) {
  v4u u;
  for (unsigned spins = 0;; ++spins) {
    u = ld_sc1(rs, off);
    if (__all(unit_ok(u, t0, t1)) || spins > SPIN_CAP) break;
    __builtin_amdgcn_s_sleep(1);
  }
  x = __uint_as_float(u.x); z = __uint_as_float(u.z);
}
#define SCHED_FENCE() __builtin_amdgcn_sched_barrier(0)
__device__ __forceinline__ void pin8(float (&v)[8]) {
  asm volatile("" : "+v"(v[0]), "+v"(v[1]), "+v"(v[2]), "+v"(v[3]), "+v"(v[4]), "+v"(v[5]), "+v"(v[6]), "+v"(v[7]) :: "memory");
  __builtin_amdgcn_sched_barrier(0);
}
__device__ __forceinline__ void pin16(float (&v)[16]) {
  asm volatile("" : "+v"(v[0]), "+v"(v[1]), "+v"(v[2]), "+v"(v[3]), "+v"(v[4]), "+v"(v[5]), "+v"(v[6]), "+v"(v[7]),
                    "+v"(v[8]), "+v"(v[9]), "+v"(v[10]), "+v"(v[11]), "+v"(v[12]), "+v"(v[13]), "+v"(v[14]), "+v"(v[15]) :: "memory");
  __builtin_amdgcn_sched_barrier(0);
}
__device__ __forceinline__ void load_slot(v4u (&R)[TK], rsrc_t rs, int vo, int so) {
#pragma unroll
  for (int i = 0; i < TK; ++i) R[i] = ld_nt(rs, vo + (i >> 2) * 4096 + (i & 3) * 1024, so);
}
template <int N, int CTRL, int BIT> __device__ __forceinline__ void halve(float (&x)[TK], int c) {
#pragma unroll
  for (int h = 0; h < N / 2; ++h) { const bool hi = c & BIT; const float a = hi ? x[h + N / 2] : x[h], o = hi ? x[h] : x[h + N / 2]; x[h] = a + dpp<CTRL>(o); }
}
__device__ __forceinline__ float reduce_scores(float (&x)[TK], int c) {
  halve<16, XOR8, 8>(x, c); halve<8, XOR7, 4>(x, c); halve<4, XOR2, 2>(x, c); halve<2, XOR1, 1>(x, c);
  return x[0];
}
template <int I> __device__ __forceinline__ void pv_all(const v4u (&V)[TK], unsigned pp, float (&acc)[8]) {
  if constexpr (I < TK) {
    const unsigned pb = (unsigned)__builtin_amdgcn_mov_dpp((int)pp, NEWBC + I, 0xf, 0xf, true);
#pragma unroll
    for (int k = 0; k < 4; ++k) {
      acc[2 * k] = dot2(__builtin_amdgcn_perm(V[I + 1][k], V[I][k], 0x05040100u), pb, acc[2 * k]);
      acc[2 * k + 1] = dot2(__builtin_amdgcn_perm(V[I + 1][k], V[I][k], 0x07060302u), pb, acc[2 * k + 1]);
    }
    pv_all<I + 2>(V, pp, acc);
  }
}
__device__ __forceinline__ unsigned now32() { return (unsigned)__builtin_amdgcn_s_memrealtime(); }
__global__ void __launch_bounds__(NTHR, 2) fused_kernel(Params p) {
  extern __shared__ __attribute__((aligned(16))) unsigned char smem[];
  LAS unsigned char* L = (LAS unsigned char*)smem;
  const unsigned tbase = now32() + DELTA0;
  const int tid = threadIdx.x, lane = tid & 63, w = __builtin_amdgcn_readfirstlane(tid >> 6);
  const int b = blockIdx.x;
  const int pair = b >> 2, sp = b & 3;
  const int br = pair >> 5, head = pair & 31;
  const int r = lane >> 4, c = lane & 15;
  const rsrc_t rsW = __builtin_amdgcn_make_buffer_rsrc(p.ws, 0, WS_END, RSRC3);

  const size_t hoff = (size_t)head * HEAD_BYTES;
  const rsrc_t rsK = __builtin_amdgcn_make_buffer_rsrc((void*)((const char*)(br ? p.kB : p.kA) + hoff), 0, HEAD_BYTES, RSRC3);
  const rsrc_t rsV = __builtin_amdgcn_make_buffer_rsrc((void*)((const char*)(br ? p.vB : p.vA) + hoff), 0, HEAD_BYTES, RSRC3);
  const int vo = lane * 16;
  v4u K[TK], V[TK];
  constexpr int NQW = 5;
  const int ph = w == 0 ? 7 : (w < NQW ? w + 2 : w - NQW);
  unsigned due256 = SIGMA256 * (unsigned)(ph * 32 + (b >> 3)) / 256u;
  int so = (sp * (WITEMS * NWAVE) + ph) * ITEM_BYTES;
  auto wait_due = [&]() __attribute__((always_inline)) {
    const unsigned d = tbase + (due256 >> 8);
    while ((int)(now32() - d) < 0) __builtin_amdgcn_s_sleep(1);
    due256 += SIGMA256;
  };
  volatile LAS unsigned* ctl = (volatile LAS unsigned*)(L + L_SH);
  const int row0 = 160 * sp + 32 * (w < NQW ? w : 0);
  v4u wv[8];
  {
    const rsrc_t rsQ = __builtin_amdgcn_make_buffer_rsrc((void*)(br ? p.wqB : p.wqA), 0, w < NQW ? DM * NH * DH * 2 : 0, RSRC3);
#pragma unroll
    for (int i = 0; i < 8; ++i) wv[i] = ld_nt(rsQ, (row0 + r) * (NH * DH * 2) + head * (DH * 2) + c * 16, i * 4 * (NH * DH * 2));
  }
  if (tid < 2) ctl[tid] = 0u;
  __syncthreads();
  SCHED_FENCE();
  if (w >= NQW) {
    const char* kp = (const char*)(br ? p.kB : p.kA) + hoff + (size_t)sp * (HEAD_BYTES / NSPLIT);
    const char* vp = (const char*)(br ? p.vB : p.vA) + hoff + (size_t)sp * (HEAD_BYTES / NSPLIT);
    const char* wp = (const char*)(br ? p.woB : p.woA) + (size_t)head * DH * DM * 2 + sp * 320;
    const char* src = w == 5 ? (lane < 32 ? vp : kp)
                    : w == 6 ? (lane < 32 ? wp : wp + (DH - 1) * DM * 2)
                             : (lane < 16 ? (const char*)p.out : lane < 32 ? (const char*)p.ws + WS_P : lane < 48 ? (const char*)p.x : (const char*)p.gw);
    const int dst = L_RED + (w - NQW) * 256;
    unsigned keep;
    asm volatile("s_mov_b32 %0, m0\n\ts_mov_b32 m0, %2\n\ts_nop 0\n\tglobal_load_lds_dword %1, off nt\n\ts_mov_b32 m0, %0"
                 : "=&s"(keep) : "v"(src), "s"(dst) : "memory");
  }
  const CAS unsigned* ep = (const CAS unsigned*)((const char*)p.ws + WS_EPOCH);
  unsigned epoch = 0;
  if (w < NQW) {
    unsigned xs[16];
    const CAS unsigned* xp = (const CAS unsigned*)p.x + row0 / 2;
#pragma unroll
    for (int i = 0; i < 16; ++i) xs[i] = xp[i];
    epoch = *ep;
    asm volatile("" : "+s"(epoch), "+s"(xs[0]), "+s"(xs[1]), "+s"(xs[2]), "+s"(xs[3]), "+s"(xs[4]), "+s"(xs[5]), "+s"(xs[6]), "+s"(xs[7]),
                      "+s"(xs[8]), "+s"(xs[9]), "+s"(xs[10]), "+s"(xs[11]), "+s"(xs[12]), "+s"(xs[13]), "+s"(xs[14]), "+s"(xs[15]));
    const unsigned tag0 = epoch + 1u, tag1 = (epoch + 1u) * 0x9E3779B1u ^ 0x5bd1e995u;
    float a[8];
    const unsigned mhi = (r & 2) ? 0xffffffffu : 0u, shl = (r & 1) ? 0u : 16u;
#pragma unroll
    for (int k = 0; k < 8; ++k) a[k] = 0.f;
#pragma unroll
    for (int i = 0; i < 8; ++i) {
      const unsigned d = xs[2 * i] ^ ((xs[2 * i] ^ xs[2 * i + 1]) & mhi);
      const float xf = __builtin_bit_cast(float, (d << shl) & 0xffff0000u);
#pragma unroll
      for (int k = 0; k < 4; ++k) { a[2 * k] = fmaf(xf, bf2f_lo(wv[i][k]), a[2 * k]); a[2 * k + 1] = fmaf(xf, bf2f_hi(wv[i][k]), a[2 * k + 1]); }
    }
#pragma unroll
    for (int k = 0; k < 8; ++k) a[k] = xrow_sum(a[k]);
    LAS float* qw_l = (LAS float*)(L + L_A) + w * REC;
    *(LAS v4f*)(qw_l + 8 * c + 4 * (r & 1)) = (r & 1) ? v4f{a[4], a[5], a[6], a[7]} : v4f{a[0], a[1], a[2], a[3]};
    asm volatile("s_waitcnt lgkmcnt(0)" ::: "memory");
    if (lane == 0) __hip_atomic_fetch_add((LAS unsigned*)(L + L_SH), 1u, __ATOMIC_RELAXED, __HIP_MEMORY_SCOPE_WORKGROUP);
    if (w == 0) {
      __builtin_amdgcn_s_setprio(3);
      while (ctl[0] < NQW) __builtin_amdgcn_s_sleep(1);
      asm volatile("" ::: "memory");
      const LAS float* s = (const LAS float*)(L + L_A) + 2 * lane;
      float v0 = 0.f, v1 = 0.f;
#pragma unroll
      for (int k = 0; k < NQW; ++k) { v0 += s[k * REC]; v1 += s[k * REC + 1]; }
      st_unit(rsW, WS_Q + (pair * 4 + sp) * Q_REC + lane * 16, v0, v1, tag0, tag1);
      __builtin_amdgcn_s_sleep(16);
      while ((int)(now32() - (tbase - DELTA0 + POLL_AT)) < 0) __builtin_amdgcn_s_sleep(1);
      const int qo = WS_Q + pair * 4 * Q_REC + lane * 16;
      v4u u0, u1, u2, u3;
      for (unsigned spins = 0;; ++spins) {
        u0 = ld_sc1(rsW, qo); u1 = ld_sc1(rsW, qo + Q_REC); u2 = ld_sc1(rsW, qo + 2 * Q_REC); u3 = ld_sc1(rsW, qo + 3 * Q_REC);
        if (__all(unit_ok(u0, tag0, tag1) && unit_ok(u1, tag0, tag1) && unit_ok(u2, tag0, tag1) && unit_ok(u3, tag0, tag1)) || spins > SPIN_CAP) break;
        __builtin_amdgcn_s_sleep(1);
      }
      const float q0 = (__uint_as_float(u0.x) + __uint_as_float(u1.x)) + (__uint_as_float(u2.x) + __uint_as_float(u3.x));
      const float q1 = (__uint_as_float(u0.z) + __uint_as_float(u1.z)) + (__uint_as_float(u2.z) + __uint_as_float(u3.z));
      ((LAS unsigned*)(L + L_C))[lane] = pack_bf(q0, q1);
      asm volatile("s_waitcnt lgkmcnt(0)" ::: "memory");
      ctl[1] = 1u;
      __builtin_amdgcn_s_setprio(0);
    }
  }
  SCHED_FENCE();
  wait_due();
  load_slot(K, rsK, vo, so);
  SCHED_FENCE();
  if (b < 40 && w == NWAVE - 1) {
    const rsrc_t rsX = __builtin_amdgcn_make_buffer_rsrc((void*)p.x, 0, DM * 2, RSRC3);
    const rsrc_t rsG = __builtin_amdgcn_make_buffer_rsrc((void*)p.gw, 0, DM * 2, RSRC3);
    unsigned gx[5], gg[5];
#pragma unroll
    for (int k = 0; k < 5; ++k) { gx[k] = __builtin_amdgcn_raw_buffer_load_b32(rsX, lane * 4, k * 256, 0); gg[k] = __builtin_amdgcn_raw_buffer_load_b32(rsG, lane * 4, k * 256, 0); }
    SCHED_FENCE();
    float gs = 0.f;
#pragma unroll
    for (int k = 0; k < 5; ++k) gs += rbf(bf2f_lo(gx[k]) * bf2f_lo(gg[k])) + rbf(bf2f_hi(gx[k]) * bf2f_hi(gg[k]));
    gs = xrow_sum(row_sum16(gs));
    ((LAS float*)(L + L_G))[0] = rbf(1.f / (1.f + __expf(-rbf(gs))));
  }
  SCHED_FENCE();
  wait_due();
  load_slot(V, rsV, vo, so);
  SCHED_FENCE();
  if (w >= NQW) { epoch = *ep; asm volatile("" : "+s"(epoch)); }
  const unsigned tag0 = epoch + 1u, tag1 = (epoch + 1u) * 0x9E3779B1u ^ 0x5bd1e995u;
  while (ctl[1] == 0u) __builtin_amdgcn_s_sleep(1);
  asm volatile("" ::: "memory");
  const v4u q = *(const LAS v4u*)(L + L_C + c * 16);

  constexpr float C2 = 0.08838834764831845f * 1.4426950408889634f;
  float m = -INFINITY, l = 0.f, acc[8];
#pragma unroll
  for (int k = 0; k < 8; ++k) acc[k] = 0.f;
  auto scores = [&](float (&x)[TK]) __attribute__((always_inline)) {
#pragma unroll
    for (int i = 0; i < TK; ++i) {
      float d = dot2(K[i][0], q[0], 0.f);
      d = dot2(K[i][1], q[1], d); d = dot2(K[i][2], q[2], d); x[i] = dot2(K[i][3], q[3], d);
    }
  };
  auto finish = [&](float (&x)[TK]) __attribute__((always_inline)) {
    const float s = reduce_scores(x, c) * C2;
    float t = fmaxf(s, dpp<XOR1>(s));
    t = fmaxf(t, dpp<XOR2>(t)); t = fmaxf(t, dpp<XOR7>(t)); t = fmaxf(t, dpp<XOR8>(t));
    t = fmaxf(t, dpp_keep<BC15, 0xa>(t)); t = fmaxf(t, dpp_keep<BC31, 0xc>(t));
    const float mt = __builtin_bit_cast(float, __builtin_amdgcn_readlane(__builtin_bit_cast(int, t), 63));
    const float mn = fmaxf(m, mt);
    const float alpha = __builtin_amdgcn_exp2f(m - mn);
    const float pr = __builtin_amdgcn_exp2f(s - mn);
    m = mn;
    l = fmaf(l, alpha, pr);
#pragma unroll
    for (int k = 0; k < 8; ++k) acc[k] *= alpha;
    pv_all<0>(V, pack_bf(pr, dpp<XOR1>(pr)), acc);
  };
  for (int it = 0; it + 1 < WITEMS; ++it) {
    so += NWAVE * ITEM_BYTES;
    float x[TK];
    scores(x);
    pin16(x);
    wait_due();
    load_slot(K, rsK, vo, so);
    SCHED_FENCE();
    finish(x);
    pin8(acc);
    wait_due();
    load_slot(V, rsV, vo, so);
    SCHED_FENCE();
  }
  {
    float x[TK];
    scores(x);
    finish(x);
  }
  {
    float h[4], g2[2];
#pragma unroll
    for (int k = 0; k < 4; ++k) {
      auto s = __builtin_amdgcn_permlane16_swap(__float_as_uint(acc[2 * k]), __float_as_uint(acc[2 * k + 1]), false, false);
      h[k] = __uint_as_float(s[0]) + __uint_as_float(s[1]);
    }
#pragma unroll
    for (int k = 0; k < 2; ++k) {
      auto s = __builtin_amdgcn_permlane32_swap(__float_as_uint(h[2 * k]), __float_as_uint(h[2 * k + 1]), false, false);
      g2[k] = __uint_as_float(s[0]) + __uint_as_float(s[1]);
    }
    l = xrow_sum(row_sum16(l));
    LAS float* rec = (LAS float*)(L + L_A) + w * REC;
    rec[4 + 8 * c + r] = g2[0]; rec[4 + 8 * c + 4 + r] = g2[1];
    *(LAS v2f*)rec = v2f{m, l};
  }
  {
    const char* wo = (const char*)(br ? p.woB : p.woA) + (size_t)head * DH * DM * 2 + sp * 320;
#pragma unroll
    for (int i = 0; i < 5; ++i) {
      const int piece = (w * 5 + i) * 64 + lane;
      const char* src = wo + (piece / 20) * (DM * 2) + (piece % 20) * 16;
      const int dst = L_WO + (w * 5 + i) * 1024;
      unsigned keep;
      asm volatile("s_mov_b32 %0, m0\n\ts_mov_b32 m0, %2\n\ts_nop 0\n\tglobal_load_lds_dwordx4 %1, off nt\n\ts_mov_b32 m0, %0"
                   : "=&s"(keep) : "v"(src), "s"(dst) : "memory");
    }
  }
  __syncthreads();
  if (w == 0) {
    const LAS float* rec = (const LAS float*)(L + L_A);
    float M = rec[0];
#pragma unroll
    for (int k = 1; k < 8; ++k) M = fmaxf(M, rec[k * REC]);
    float Ls = 0.f, o0 = 0.f, o1 = 0.f;
#pragma unroll
    for (int k = 0; k < 8; ++k) {
      const float wt = __builtin_amdgcn_exp2f(rec[k * REC] - M);
      Ls = fmaf(wt, rec[k * REC + 1], Ls); o0 = fmaf(wt, rec[k * REC + 4 + 2 * lane], o0); o1 = fmaf(wt, rec[k * REC + 5 + 2 * lane], o1);
    }
    const int po = WS_P + (pair * 4 + sp) * P_REC;
    st_unit(rsW, po + lane * 16, o0, o1, tag0, tag1);
    st_unit(rsW, lane == 0 ? po + 64 * 16 : OOB, M, Ls, tag0, tag1);
  }
  float so16[16];
  {
    const int pb = WS_P + pair * 4 * P_REC;
    const int po = lane < 32 ? pb + (lane >> 3) * P_REC + (8 * w + (lane & 7)) * 16 : pb + (lane & 3) * P_REC + 64 * 16;
    float ux, uz;
    poll_unit(rsW, po, tag0, tag1, ux, uz);
    float mk[4], lk[4];
#pragma unroll
    for (int k = 0; k < 4; ++k) {
      mk[k] = __builtin_bit_cast(float, __builtin_amdgcn_readlane(__builtin_bit_cast(int, ux), 32 + k));
      lk[k] = __builtin_bit_cast(float, __builtin_amdgcn_readlane(__builtin_bit_cast(int, uz), 32 + k));
    }
    const float M = fmaxf(fmaxf(mk[0], mk[1]), fmaxf(mk[2], mk[3]));
    const float mine = (lane & 16) ? ((lane & 8) ? mk[3] : mk[2]) : ((lane & 8) ? mk[1] : mk[0]);
    const float Ls = fmaf(__builtin_amdgcn_exp2f(mk[0] - M), lk[0], __builtin_amdgcn_exp2f(mk[1] - M) * lk[1]) +
                     fmaf(__builtin_amdgcn_exp2f(mk[2] - M), lk[2], __builtin_amdgcn_exp2f(mk[3] - M) * lk[3]);
    const float wt = __builtin_amdgcn_exp2f(mine - M) * __builtin_amdgcn_rcpf(Ls);
    float v0 = wt * ux, v1 = wt * uz;
    v0 += dpp<XOR8>(v0); v1 += dpp<XOR8>(v1);
    auto s0 = __builtin_amdgcn_permlane16_swap(__float_as_uint(v0), __float_as_uint(v0), false, false);
    auto s1 = __builtin_amdgcn_permlane16_swap(__float_as_uint(v1), __float_as_uint(v1), false, false);
    v0 = __uint_as_float(s0[0]) + __uint_as_float(s0[1]); v1 = __uint_as_float(s1[0]) + __uint_as_float(s1[1]);
#pragma unroll
    for (int u8 = 0; u8 < 8; ++u8) {
      so16[2 * u8] = __builtin_bit_cast(float, __builtin_amdgcn_readlane(__builtin_bit_cast(int, v0), u8));
      so16[2 * u8 + 1] = __builtin_bit_cast(float, __builtin_amdgcn_readlane(__builtin_bit_cast(int, v1), u8));
    }
  }
  asm volatile("" ::: "memory");
  {
    const int cl = lane < 40 ? lane : 39;
    float a[4] = {0.f, 0.f, 0.f, 0.f};
#pragma unroll
    for (int i = 0; i < 16; ++i) {
      const v2u wv2 = *(const LAS v2u*)(L + L_WO + (16 * w + i) * 320 + cl * 8);
      a[0] = fmaf(so16[i], bf2f_lo(wv2[0]), a[0]); a[1] = fmaf(so16[i], bf2f_hi(wv2[0]), a[1]);
      a[2] = fmaf(so16[i], bf2f_lo(wv2[1]), a[2]); a[3] = fmaf(so16[i], bf2f_hi(wv2[1]), a[3]);
    }
    if (lane < 40) *(LAS v4f*)((LAS float*)(L + L_RED) + w * 160 + 4 * lane) = v4f{a[0], a[1], a[2], a[3]};
  }
  __syncthreads();
  if (tid < 80) {
    const LAS float* red = (const LAS float*)(L + L_RED) + 2 * tid;
    float y0 = 0.f, y1 = 0.f;
#pragma unroll
    for (int k = 0; k < 8; ++k) { y0 += red[k * 160]; y1 += red[k * 160 + 1]; }
    st_unit(rsW, WS_Y + pair * Y_REC + (sp * 80 + tid) * 16, y0, y1, tag0, tag1);
  }
  if (b < 40) {
    const int yo = WS_Y + (tid >> 3) * Y_REC + (b * 8 + (tid & 7)) * 16;
    float y0, y1;
    poll_unit(rsW, yo, tag0, tag1, y0, y1);
    y0 += dpp<XOR8>(y0); y1 += dpp<XOR8>(y1);
    y0 = xrow_sum(y0); y1 = xrow_sum(y1);
    LAS float* red = (LAS float*)(L + L_C) + w * 16;
    if (lane < 8) *(LAS v2f*)(red + 2 * lane) = v2f{y0, y1};
    __syncthreads();
    if (tid < 8) {
      const LAS float* rr = (const LAS float*)(L + L_C) + 2 * tid;
      const float gate = ((const LAS float*)(L + L_G))[0], ngate = rbf(1.f - gate);
      float a0 = (rr[0] + rr[16]) + (rr[32] + rr[48]), a1 = (rr[1] + rr[17]) + (rr[33] + rr[49]);
      float b0 = (rr[64] + rr[80]) + (rr[96] + rr[112]), b1 = (rr[65] + rr[81]) + (rr[97] + rr[113]);
      const float r0 = rbf(gate * rbf(a0)) + rbf(ngate * rbf(b0)), r1 = rbf(gate * rbf(a1)) + rbf(ngate * rbf(b1));
      ((GAS unsigned*)p.out)[b * 8 + tid] = pack_bf(r0, r1);
      if (b == 0 && tid == 0) __hip_atomic_store((GAS unsigned*)((char*)p.ws + WS_EPOCH), tag0, __ATOMIC_RELAXED, __HIP_MEMORY_SCOPE_AGENT);
    }
  }
}
}

extern "C" void kernel_launch(void* const* d_in, const int* in_sizes, int n_in,
                              void* d_out, int out_size, void* d_ws, size_t ws_size,
                              hipStream_t stream) {
  Params p;
  p.x = d_in[0]; p.kA = d_in[1]; p.vA = d_in[2]; p.wqA = d_in[3]; p.woA = d_in[4];
  p.kB = d_in[6]; p.vB = d_in[7]; p.wqB = d_in[8]; p.woB = d_in[9]; p.gw = d_in[10];
  p.out = d_out; p.ws = d_ws;
  hipLaunchKernelGGL(fused_kernel, dim3(NWG), dim3(NTHR), L_END, stream, p);
}
```
